```python
import torch

def _softmax(x, axis=-1):
    return torch.softmax(x, dim=axis)
def _gelu(x):
    return torch.nn.functional.gelu(x, approximate="tanh")
def _silu(x):
    return torch.nn.functional.silu(x)
def _sigmoid(x):
    return torch.sigmoid(x)
def _layernorm(x, axis=-1, eps=1e-5):
    m = x.mean(dim=axis, keepdim=True)
    v = x.var(dim=axis, keepdim=True, unbiased=False)
    return (x - m) / torch.sqrt(v + eps)
def _scatter_add(tgt, idx, src, axis=0):
    return tgt.clone().index_add_(axis, idx, src)
def _segment_sum(src, seg, n):
    out = torch.zeros((n,) + src.shape[1:], dtype=src.dtype)
    return out.index_add_(0, seg, src)
def _pad(x, pw):
    flat = tuple(v for pair in reversed(pw) for v in pair)
    return torch.nn.functional.pad(x, flat)

kernel_name = 'recurrent_scan__mha_block__moe_arch_mod__prompt'

def setup_inputs():

    _w = (lambda a: (lambda t: t.to(torch.bfloat16) if t.is_floating_point() else t)(torch.as_tensor(a)))
    def _A_setup():
        B, S, D, H = 1, 2048, 448, 16
        return {
            "x": _w((lambda *s: torch.randn(*s, dtype=torch.bfloat16) * 0.05)(B, S, D)),
            "wqkv": _w((lambda *s: torch.randn(*s, dtype=torch.bfloat16) * 0.05)(D, 3 * D)),
            "wo": _w((lambda *s: torch.randn(*s, dtype=torch.bfloat16) * 0.05)(D, D)),
        }
    _A_in = _A_setup()

    def _B_setup():
        B, S, D, F, C = 1, 2048, 448, 1792, 1
        return {
            "x": _w((lambda *s: torch.randn(*s, dtype=torch.bfloat16) * 0.05)(B, S, D)),
            "w_r": _w((lambda *s: torch.randn(*s, dtype=torch.bfloat16) * 0.05)(D)),
            "w_gate": _w((lambda *s: torch.randn(*s, dtype=torch.bfloat16) * 0.05)(D, F)),
            "w_up": _w((lambda *s: torch.randn(*s, dtype=torch.bfloat16) * 0.05)(D, F)),
            "w_down": _w((lambda *s: torch.randn(*s, dtype=torch.bfloat16) * 0.05)(F, D)),
        }
    _B_in = _B_setup()

    return {
        "A_x": _A_in["x"],
        "A_wqkv": _A_in["wqkv"],
        "A_wo": _A_in["wo"],
        "B_x": _B_in["x"],
        "B_w_r": _B_in["w_r"],
        "B_w_gate": _B_in["w_gate"],
        "B_w_up": _B_in["w_up"],
        "B_w_down": _B_in["w_down"],
    }

def reference(A_x, A_wqkv, A_wo, B_x, B_w_r, B_w_gate, B_w_up, B_w_down):
    def _A_ref(x, wqkv, wo):
        B, S, D = x.shape
        H = 16
        Dh = D // H
        qkv = (lambda x, *d: x.permute(*d))((x @ wqkv).reshape(B, S, 3, H, Dh), 2, 0, 3, 1, 4)
        q, k, v = qkv[0], qkv[1], qkv[2]
        scores = torch.einsum("bhqd,bhkd->bhqk", q, k) * (Dh ** -0.5)
        p = _softmax(scores, axis=-1)
        o = torch.einsum("bhqk,bhkd->bhqd", p, v)
        return (lambda x, *d: x.permute(*d))(o, 0, 2, 1, 3).reshape(B, S, D) @ wo

    def _B_ref(x, w_r, w_gate, w_up, w_down):
        B, S, D = x.shape
        C = 1
        r = x @ w_r
        idx = torch.argsort(-r, axis=-1)[:, :C]
        idx_e = (lambda x, n, axis: torch.repeat_interleave(x, n, dim=axis))(idx.reshape(B, C, 1), D, axis=2)
        x_sel = (lambda x, i, axis: torch.take_along_dim(x, i, dim=axis))(x, idx_e, axis=1)
        g = _silu(x_sel @ w_gate)
        u = x_sel @ w_up
        y = (g * u) @ w_down
        r_sel = _sigmoid((lambda x, i, axis: torch.take_along_dim(x, i, dim=axis))(r, idx, axis=1))
        y = y * r_sel.reshape(B, C, 1)
        b_off = (torch.arange(B) * S).reshape(B, 1)
        flat_idx = (idx + b_off).reshape(B * C)
        zero = (x * 0.0).reshape(B * S, D)
        upd = _scatter_add(zero, flat_idx, y.reshape(B * C, D), axis=0)
        return x + upd.reshape(B, S, D)

    x = A_x
    xn = _layernorm(x, axis=-1)
    s = _A_ref(xn, A_wqkv, A_wo)
    g = _silu(_B_ref(xn, B_w_r, B_w_gate, B_w_up, B_w_down))
    return x + s * g
```

```cpp
#include <hip/hip_runtime.h>
#include <hip/hip_bf16.h>
#include <stdint.h>

typedef __attribute__((ext_vector_type(8))) __bf16 bf8;
typedef __attribute__((ext_vector_type(2))) __bf16 bf2;
typedef __attribute__((ext_vector_type(16))) float f16v;
typedef __attribute__((ext_vector_type(4))) float f4v;
typedef __attribute__((ext_vector_type(2))) float f2v;
typedef __attribute__((ext_vector_type(4))) unsigned u4v;
typedef __attribute__((ext_vector_type(2))) unsigned u2v;
typedef __attribute__((ext_vector_type(4))) short s4v;
typedef __attribute__((address_space(3))) s4v lds_s4v;
typedef unsigned short u16;

#define DI __device__ __forceinline__
#define SGB(M, N) __builtin_amdgcn_sched_group_barrier(M, N, 0)
#define STAMP(WS, KID, IDX) do { } while (0)

constexpr int S_ = 2048, D_ = 448, H_ = 16, DH_ = 28, F_ = 1792, N3_ = 1344;
constexpr int NFFN = 28;

constexpr size_t MB_ = 1024 * 1024;
constexpr size_t OFF_Q = 0, OFF_K = 2 * MB_, OFF_V = 4 * MB_, OFF_O = 6 * MB_;
constexpr size_t OFF_STATS = 8 * MB_;
constexpr size_t OFF_R = OFF_STATS + 2048 * 8;
constexpr size_t OFF_KMAX = OFF_R + 2048 * 4;
constexpr size_t OFF_YPART = OFF_KMAX + 1024;
constexpr size_t OFF_Y = OFF_YPART + NFFN * 448 * 4;
constexpr size_t OFF_CTRL = OFF_Y + 448 * 4 + 256;

constexpr float QSCALE = 0.18898223650461362f * 1.4426950408889634f;
constexpr float L_THRESH = 9.094947e-13f;

DI float bf16r(float x) { unsigned u = __float_as_uint(x); u += 0x7fffu + ((u >> 16) & 1u); return __uint_as_float(u & 0xffff0000u); }
DI unsigned pk2(float lo, float hi) { bf2 r = __builtin_convertvector((f2v){lo, hi}, bf2); return __builtin_bit_cast(unsigned, r); }
DI float bflo(unsigned u) { return __uint_as_float(u << 16); }
DI float bfhi(unsigned u) { return __uint_as_float(u & 0xffff0000u); }
DI float dot2(unsigned a, unsigned b, float c) { return __builtin_amdgcn_fdot2_f32_bf16(__builtin_bit_cast(bf2, a), __builtin_bit_cast(bf2, b), c, false); }
DI f16v mfma32(bf8 a, bf8 b, f16v c) { return __builtin_amdgcn_mfma_f32_32x32x16_bf16(a, b, c, 0, 0, 0); }
DI int crow(int r, int h) { return (r & 3) + 8 * (r >> 2) + 4 * h; }
DI float swap32(float v) { return __shfl_xor(v, 32); }
DI bf8 tr_frag(const char* lds_base, int off) {
  s4v a = __builtin_amdgcn_ds_read_tr16_b64_v4i16((lds_s4v*)(lds_base + off));
  s4v b = __builtin_amdgcn_ds_read_tr16_b64_v4i16((lds_s4v*)(lds_base + off + 4 * 192));
  typedef __attribute__((ext_vector_type(8))) short s8v;
  s8v r = __builtin_shufflevector(a, b, 0, 1, 2, 3, 4, 5, 6, 7);
  return __builtin_bit_cast(bf8, r);
}

constexpr int K1_WROW = 192;
constexpr int K1_LDS_W = 448 * K1_WROW;
constexpr int K1_LDS_X = 2 * 128 * 128;
constexpr int K1_LDS = K1_LDS_W + K1_LDS_X + 3 * 32 * 4 + 4 * 32 * 2 * 4 + 16 + 4 * 96 * 4;

__global__ __launch_bounds__(512, 2) void k1_lnqkv(const u16* __restrict__ x, const u16* __restrict__ wqkv,
                                                   const u16* __restrict__ wr, char* __restrict__ ws) {
  extern __shared__ __attribute__((aligned(16))) char smem[];
  char* Wl = smem;
  char* Xl = smem + K1_LDS_W;
  float* cs_l = (float*)(smem + K1_LDS_W + K1_LDS_X);
  float* st_l = cs_l + 96;
  float* km_l = st_l + 256;
  float* cs_p = km_l + 4;
  const int tid = threadIdx.x, lane = tid & 63, wave = tid >> 6;
  const int l31 = lane & 31, hh = lane >> 5;
  const int role = wave >> 2, tw = wave & 3;
  const int b = blockIdx.x, xcd = b & 7, j = b >> 3;
  const int mblk = (xcd & 3) + 4 * (j & 3), head = (xcd >> 2) * 8 + (j >> 2);
  const int tok = mblk * 128 + tw * 32 + l31;

  if (b == 0 && tid == 0) { ((int*)(ws + OFF_CTRL))[0] = 0; }
  STAMP(ws, 0, 0);

  const int coff = (head & 1) * 4;
  const int rpc = (head & 1) ? 4 : 3;
  u4v wt[14], xt[14];
  unsigned wrv[14];
  {
    const u16* xb = x + (size_t)(mblk * 128 + wave * 16 + (lane >> 3)) * D_ + (lane & 7) * 8;
#pragma unroll
    for (int c = 0; c < 7; ++c) {
#pragma unroll
      for (int i = 0; i < 2; ++i) {
        const int idx = (tid + 512 * i) & 1023;
        const int idc = idx < 768 ? idx : 0;
        const int kl = idc / 12, rem = idc - kl * 12, pp = rem >> 2, cc = rem & 3;
        wt[c * 2 + i] = *(const u4v*)(wqkv + (size_t)(c * 64 + kl) * N3_ + pp * D_ + (head * DH_ & ~7) + 8 * cc);
        wrv[c * 2 + i] = (unsigned)wr[c * 64 + kl];
      }
#pragma unroll
      for (int i = 0; i < 2; ++i) xt[c * 2 + i] = *(const u4v*)(xb + (size_t)i * 8 * D_ + c * 64);
    }
  }
  const int xrow_w = wave * 16 + (lane >> 3);
#define CWRITE(C) do { \
    _Pragma("unroll") for (int i = 0; i < 2; ++i) { const int idx = tid + 512 * i; \
      if (idx < 768) { const int kl = idx / 12, rem = idx - kl * 12, pp = rem >> 2, cc = rem & 3; \
        u4v v_ = wt[(C) * 2 + i]; \
        { const unsigned m_ = (rem == rpc) ? 0xffff0000u : 0u; const unsigned pw_ = wrv[(C) * 2 + i] << 16; \
          if (head & 1) v_[0] = (v_[0] & ~m_) | (pw_ & m_); else v_[2] = (v_[2] & ~m_) | (pw_ & m_); } \
        *(u4v*)(Wl + ((C) * 64 + kl) * K1_WROW + pp * 64 + cc * 16) = v_; } } \
    _Pragma("unroll") for (int i = 0; i < 2; ++i) { const int row_ = xrow_w + 8 * i; \
      *(u4v*)(Xl + ((C) & 1) * 16384 + row_ * 128 + (((lane & 7) ^ ((row_ >> 1) & 7)) * 16)) = xt[(C) * 2 + i]; } } while (0)
  CWRITE(0);
  STAMP(ws, 0, 1);
  __syncthreads();
  STAMP(ws, 0, 2);

  const char* trb = Wl + (8 * hh + ((lane & 15) >> 2)) * K1_WROW + (coff + 16 * ((lane >> 4) & 1) + 4 * (lane & 3)) * 2;
  const int xrow_r = tw * 32 + l31;
  const char* xrb = Xl + xrow_r * 128;
  const int xsw = (xrow_r >> 1) & 7;
  f16v accA = {}, accB = {};
  float sum = 0.f, sq = 0.f, c0 = 0.f, c1 = 0.f, c2 = 0.f;
#pragma unroll
  for (int c = 0; c < 7; ++c) {
    if (c + 1 < 7) CWRITE(c + 1);
    if (role == 0) {
#pragma unroll
      for (int sl = 0; sl < 4; ++sl) {
        const int s_ = c * 4 + sl;
        const bf8 xf = *(const bf8*)(xrb + (c & 1) * 16384 + (((2 * sl + hh) ^ xsw) * 16));
        const bf8 w0 = tr_frag(trb, s_ * 16 * K1_WROW);
        const bf8 w1 = tr_frag(trb, s_ * 16 * K1_WROW + 64);
        accA = mfma32(w0, xf, accA);
        accB = mfma32(w1, xf, accB);
      }
    } else {
#pragma unroll
      for (int sl = 0; sl < 4; ++sl) {
        const int s_ = c * 4 + sl;
        const bf8 xf = *(const bf8*)(xrb + (c & 1) * 16384 + (((2 * sl + hh) ^ xsw) * 16));
        const bf8 w2 = tr_frag(trb, s_ * 16 * K1_WROW + 128);
        accA = mfma32(xf, w2, accA);
        const u4v xw = __builtin_bit_cast(u4v, xf);
#pragma unroll
        for (int i = 0; i < 4; ++i) { sum = dot2(xw[i], 0x3f803f80u, sum); sq = dot2(xw[i], xw[i], sq); }
        if (tw == sl) {
          const u4v u0 = __builtin_bit_cast(u4v, tr_frag(trb, s_ * 16 * K1_WROW));
          const u4v u1 = __builtin_bit_cast(u4v, tr_frag(trb, s_ * 16 * K1_WROW + 64));
          const u4v u2 = __builtin_bit_cast(u4v, w2);
#pragma unroll
          for (int i = 0; i < 4; ++i) { c0 = dot2(u0[i], 0x3f803f80u, c0); c1 = dot2(u1[i], 0x3f803f80u, c1); c2 = dot2(u2[i], 0x3f803f80u, c2); }
        }
      }
    }
    if (c + 1 < 7) __syncthreads();
  }
#undef CWRITE
  STAMP(ws, 0, 3);
  if (role == 1) {
    sum += swap32(sum); sq += swap32(sq);
    const float mean = sum * (1.0f / 448.0f);
    const float var = fmaxf(sq * (1.0f / 448.0f) - mean * mean, 0.f);
    const float m_b = bf16r(mean);
    const float sg_b = bf16r(sqrtf(bf16r(bf16r(var) + 1e-5f)));
    const float rstd = 1.0f / sg_b;
    if (hh == 0) {
      st_l[(tw * 32 + l31) * 2] = m_b; st_l[(tw * 32 + l31) * 2 + 1] = rstd;
      if (head == 0) *(f2v*)(ws + OFF_STATS + (size_t)tok * 8) = (f2v){m_b, rstd};
    }
    c0 += swap32(c0); c1 += swap32(c1); c2 += swap32(c2);
    if (hh == 0) { cs_p[tw * 96 + l31] = c0; cs_p[tw * 96 + 32 + l31] = c1; cs_p[tw * 96 + 64 + l31] = c2; }
  }
  __syncthreads();
  if (tid < 96) cs_l[tid] = (cs_p[tid] + cs_p[96 + tid]) + (cs_p[192 + tid] + cs_p[288 + tid]);
  __syncthreads();
  STAMP(ws, 0, 4);

  const int tile = mblk * 4 + tw;
  const size_t fbase = ((size_t)(head * 64 + tile) * 2) * 64 * 16 + lane * 16;
  if (role == 0) {
    const f2v st = *(const f2v*)&st_l[(tw * 32 + l31) * 2];
    const float m_b = st[0], rstd = st[1];
    {
      float v[16];
#pragma unroll
      for (int r = 0; r < 16; ++r) v[r] = accA[r] - m_b * cs_l[crow(r, hh)];
      if (hh == 1) {
        if (head == 0) ((float*)(ws + OFF_R))[tok] = bf16r(v[13] * rstd);
        v[12] = 0.f; v[13] = 0.f; v[14] = 0.f; v[15] = 0.f;
      }
      const float sc = rstd * QSCALE;
      unsigned w[8];
#pragma unroll
      for (int i = 0; i < 8; ++i) w[i] = pk2(v[2 * i] * sc, v[2 * i + 1] * sc);
#pragma unroll
      for (int s = 0; s < 2; ++s) {
        auto r0 = __builtin_amdgcn_permlane32_swap(w[4 * s + 0], w[4 * s + 2], false, false);
        auto r1 = __builtin_amdgcn_permlane32_swap(w[4 * s + 1], w[4 * s + 3], false, false);
        *(u4v*)(ws + OFF_Q + fbase + s * 1024) = (u4v){r0[0], r1[0], r0[1], r1[1]};
      }
    }
    float kn2 = 0.f;
    {
      float v[16];
#pragma unroll
      for (int r = 0; r < 16; ++r) { v[r] = (accB[r] - m_b * cs_l[32 + crow(r, hh)]) * rstd; }
      if (hh == 1) { v[12] = 0.f; v[13] = 0.f; v[14] = 0.f; v[15] = 0.f; }
#pragma unroll
      for (int r = 0; r < 16; ++r) kn2 = fmaf(v[r], v[r], kn2);
      if (hh == 1) { v[12] = 1.f; v[13] = 1.f; }
      unsigned w[8];
#pragma unroll
      for (int i = 0; i < 8; ++i) w[i] = pk2(v[2 * i], v[2 * i + 1]);
#pragma unroll
      for (int s = 0; s < 2; ++s) {
        auto r0 = __builtin_amdgcn_permlane32_swap(w[4 * s + 0], w[4 * s + 2], false, false);
        auto r1 = __builtin_amdgcn_permlane32_swap(w[4 * s + 1], w[4 * s + 3], false, false);
        *(u4v*)(ws + OFF_K + fbase + s * 1024) = (u4v){r0[0], r1[0], r0[1], r1[1]};
      }
    }
    kn2 += swap32(kn2);
#pragma unroll
    for (int o = 16; o >= 1; o >>= 1) kn2 = fmaxf(kn2, __shfl_xor(kn2, o));
    if (lane == 0) km_l[tw] = kn2;
  } else {
    const float csv = cs_l[64 + l31];
    float v[16];
#pragma unroll
    for (int r = 0; r < 16; ++r) {
      const f2v st = *(const f2v*)&st_l[(tw * 32 + crow(r, hh)) * 2];
      v[r] = (accA[r] - st[0] * csv) * st[1];
    }
    if (l31 >= DH_) {
      const float pv = (l31 == DH_) ? 1.f : 0.f;
#pragma unroll
      for (int r = 0; r < 16; ++r) v[r] = pv;
    }
#pragma unroll
    for (int s = 0; s < 2; ++s) {
      u4v o;
#pragma unroll
      for (int i = 0; i < 4; ++i) o[i] = pk2(v[8 * s + 2 * i], v[8 * s + 2 * i + 1]);
      *(u4v*)(ws + OFF_V + fbase + s * 1024) = o;
    }
  }
  __syncthreads();
  if (tid == 0) ((float*)(ws + OFF_KMAX))[head * 16 + mblk] = fmaxf(fmaxf(km_l[0], km_l[1]), fmaxf(km_l[2], km_l[3]));
  STAMP(ws, 0, 5);
}

template <int NT>
DI void ffn_block(int fb, const u16* __restrict__ x, const u16* __restrict__ wg, const u16* __restrict__ wu,
                  const u16* __restrict__ wd, char* __restrict__ ws, char* smem) {
  const int tid = threadIdx.x, lane = tid & 63, wave = tid >> 6;
  constexpr int NW = NT / 64;
  constexpr int NRS = NT / 16;
  constexpr int NGI = 448 / NRS;
  constexpr int NJS = NT / 64;
  constexpr int NDI = 64 / NJS;
  float* red_v = (float*)smem;
  int* red_i = (int*)(smem + 64);
  float* xs = (float*)(smem + 128);
  float* part = xs + 448;
  float* hbuf = part + 4096;
  __builtin_amdgcn_s_setprio(3);
  STAMP(ws, 1, 0);
  const float* rb = (const float*)(ws + OFF_R);
  float rv[S_ / NT];
#pragma unroll
  for (int i = 0; i < S_ / NT; ++i) rv[i] = rb[tid + NT * i];
  const int cg = tid & 7, mat = (tid >> 3) & 1, rs = tid >> 4;
  u4v wa[NGI];
  {
    const u16* wb = (mat ? wu : wg) + fb * 64 + cg * 8;
#pragma unroll
    for (int i = 0; i < NGI; ++i) wa[i] = *(const u4v*)(wb + (size_t)(rs + NRS * i) * F_);
  }
  const int ng = tid % 56, js = tid / 56;
  u4v wdn[NDI];
  {
    const int jc = js < NJS ? js : 0;
#pragma unroll
    for (int i = 0; i < NDI; ++i) wdn[i] = *(const u4v*)(wd + (size_t)(fb * 64 + jc + NJS * i) * D_ + ng * 8);
  }
  float bv = -INFINITY; int bi = 0x7fffffff;
#pragma unroll
  for (int i = 0; i < S_ / NT; ++i) { if (rv[i] > bv) { bv = rv[i]; bi = tid + NT * i; } }
#pragma unroll
  for (int o = 32; o >= 1; o >>= 1) {
    const float ov = __shfl_xor(bv, o); const int oi = __shfl_xor(bi, o);
    if (ov > bv || (ov == bv && oi < bi)) { bv = ov; bi = oi; }
  }
  if (lane == 0) { red_v[wave] = bv; red_i[wave] = bi; }
  __syncthreads();
  bv = red_v[0]; bi = red_i[0];
#pragma unroll
  for (int w = 1; w < NW; ++w) { const float ov = red_v[w]; const int oi = red_i[w]; if (ov > bv || (ov == bv && oi < bi)) { bv = ov; bi = oi; } }
  if (bi == 0x7fffffff) bi = 0;
  const int sel = bi;
  STAMP(ws, 1, 1);
  const f2v st = *(const f2v*)(ws + OFF_STATS + (size_t)sel * 8);
  for (int k = tid; k < D_; k += NT) xs[k] = bf16r(bf16r(bflo(x[(size_t)sel * D_ + k]) - st[0]) * st[1]);
  __syncthreads();
  {
    float a[8];
#pragma unroll
    for (int c = 0; c < 8; ++c) a[c] = 0.f;
#pragma unroll
    for (int i = 0; i < NGI; ++i) {
      const float xv = xs[rs + NRS * i];
#pragma unroll
      for (int q = 0; q < 4; ++q) { a[2 * q] = fmaf(xv, bflo(wa[i][q]), a[2 * q]); a[2 * q + 1] = fmaf(xv, bfhi(wa[i][q]), a[2 * q + 1]); }
    }
#pragma unroll
    for (int c = 0; c < 8; ++c) part[rs * 128 + mat * 64 + cg * 8 + c] = a[c];
  }
  __syncthreads();
  if (tid < 64) {
    float g = 0.f, u = 0.f;
#pragma unroll
    for (int r = 0; r < NRS; ++r) { g += part[r * 128 + tid]; u += part[r * 128 + 64 + tid]; }
    g = bf16r(g); u = bf16r(u);
    const float sl = bf16r(g / (1.0f + __expf(-g)));
    hbuf[tid] = bf16r(sl * u);
  }
  __syncthreads();
  STAMP(ws, 1, 2);
  if (js < NJS) {
    float a[8];
#pragma unroll
    for (int c = 0; c < 8; ++c) a[c] = 0.f;
#pragma unroll
    for (int i = 0; i < NDI; ++i) {
      const float hv = hbuf[js + NJS * i];
#pragma unroll
      for (int q = 0; q < 4; ++q) { a[2 * q] = fmaf(hv, bflo(wdn[i][q]), a[2 * q]); a[2 * q + 1] = fmaf(hv, bfhi(wdn[i][q]), a[2 * q + 1]); }
    }
#pragma unroll
    for (int c = 0; c < 8; ++c) part[js * 448 + ng * 8 + c] = a[c];
  }
  __syncthreads();
  float* yp = (float*)(ws + OFF_YPART) + fb * D_;
  for (int n = tid; n < D_; n += NT) {
    float sacc = 0.f;
#pragma unroll
    for (int q = 0; q < NJS; ++q) sacc += part[q * 448 + n];
    __hip_atomic_store(&yp[n], sacc, __ATOMIC_RELAXED, __HIP_MEMORY_SCOPE_AGENT);
  }
  STAMP(ws, 1, 3);
  asm volatile("s_waitcnt vmcnt(0)" ::: "memory");
  __syncthreads();
  int* ctrl = (int*)(ws + OFF_CTRL);
  if (tid == 0) {
    __builtin_amdgcn_fence(__ATOMIC_RELEASE, "agent");
    asm volatile("s_waitcnt vmcnt(0)" ::: "memory");
    const int t = __hip_atomic_fetch_add(&ctrl[0], 1, __ATOMIC_RELAXED, __HIP_MEMORY_SCOPE_AGENT);
    red_i[0] = t;
  }
  __syncthreads();
  if (red_i[0] == NFFN - 1) {
    if (tid == 0) { __builtin_amdgcn_fence(__ATOMIC_ACQUIRE, "agent"); asm volatile("s_waitcnt vmcnt(0)" ::: "memory"); }
    __syncthreads();
    const float rsel = bf16r(1.0f / (1.0f + __expf(-bv)));
    const float* ypa = (const float*)(ws + OFF_YPART);
    for (int n = tid; n < D_; n += NT) {
      float v[NFFN];
#pragma unroll
      for (int f = 0; f < NFFN; ++f) v[f] = __hip_atomic_load(&ypa[f * D_ + n], __ATOMIC_RELAXED, __HIP_MEMORY_SCOPE_AGENT);
      float sacc = 0.f;
#pragma unroll
      for (int f = 0; f < NFFN; ++f) sacc += v[f];
      ((float*)(ws + OFF_Y))[n] = bf16r(bf16r(sacc) * rsel);
    }
    if (tid == 0) ctrl[1] = sel;
  }
}

template <int QT>
__global__ __launch_bounds__(QT == 2 ? 512 : 256, 2)
void k2_attn(const u16* __restrict__ x, const u16* __restrict__ wg, const u16* __restrict__ wu,
             const u16* __restrict__ wd, char* __restrict__ ws) {
  extern __shared__ __attribute__((aligned(16))) char smem[];
  constexpr int NT = (4 / QT) * 4 * 64;
  if (blockIdx.x >= 256) { ffn_block<NT>(blockIdx.x - 256, x, wg, wu, wd, ws, smem); return; }
  const int tid = threadIdx.x, lane = tid & 63, wave = tid >> 6;
  const int l31 = lane & 31, hh = lane >> 5;
  const int b = blockIdx.x, xcd = b & 7, j = b >> 3;
  const int head = xcd * 2 + (j & 1), qblk = j >> 1;
  const int split = wave & 3, qg = wave >> 2;
  float* ol = (float*)smem;
  int* flag_l = (int*)(smem + 65536);

  STAMP(ws, 1, 0);
  bf8 qf[QT][2];
#pragma unroll
  for (int t = 0; t < QT; ++t) {
    const char* qp = ws + OFF_Q + ((size_t)(head * 64 + qblk * 4 + qg * QT + t) * 2) * 1024 + lane * 16;
    qf[t][0] = *(const bf8*)qp; qf[t][1] = *(const bf8*)(qp + 1024);
  }
  float kmax2 = 0.f;
  {
    const float* km = (const float*)(ws + OFF_KMAX) + head * 16;
#pragma unroll
    for (int i = 0; i < 16; ++i) kmax2 = fmaxf(kmax2, km[i]);
  }
  float mq[QT];
#pragma unroll
  for (int t = 0; t < QT; ++t) {
    const u4v a = __builtin_bit_cast(u4v, qf[t][0]), c = __builtin_bit_cast(u4v, qf[t][1]);
    float n2 = 0.f;
#pragma unroll
    for (int i = 0; i < 4; ++i) { n2 = dot2(a[i], a[i], n2); n2 = dot2(c[i], c[i], n2); }
    n2 += swap32(n2);
    mq[t] = sqrtf(n2 * kmax2);
  }
  const char* kbase = ws + OFF_K + ((size_t)(head * 64 + split * 16) * 2) * 1024 + lane * 16;
  const char* vbase = ws + OFF_V + ((size_t)(head * 64 + split * 16) * 2) * 1024 + lane * 16;

  for (int attempt = 0; attempt < 2; ++attempt) {
    bf8 qs1[QT];
#pragma unroll
    for (int t = 0; t < QT; ++t) {
      const float mh = bf16r(mq[t]);
      const float ml = bf16r(mq[t] - mh);
      u4v c = __builtin_bit_cast(u4v, qf[t][1]);
      if (hh == 1) c[2] = pk2(-mh, -ml);
      qs1[t] = __builtin_bit_cast(bf8, c);
    }
    f16v oacc[QT];
#pragma unroll
    for (int t = 0; t < QT; ++t) oacc[t] = (f16v){};
    bf8 kA[4], kB[4], kC[4];
#define LDKV(R, T) do { const int tt_ = ((T) < 16) ? (T) : 15; \
      R[0] = *(const bf8*)(kbase + tt_ * 2048); R[1] = *(const bf8*)(kbase + tt_ * 2048 + 1024); \
      R[2] = *(const bf8*)(vbase + tt_ * 2048); R[3] = *(const bf8*)(vbase + tt_ * 2048 + 1024); } while (0)
#define QK(R, T) mfma32(R[1], qs1[T], mfma32(R[0], qf[T][0], (f16v){}))
#define STEP(RC, RN) do { _Pragma("unroll") for (int t = 0; t < QT; ++t) { \
        const f16v sn = (t + 1 < QT) ? QK(RC, (t + 1 < QT) ? t + 1 : 0) : QK(RN, 0); \
        float e[16]; \
        _Pragma("unroll") for (int i = 0; i < 16; ++i) e[i] = __builtin_amdgcn_exp2f(sc[i]); \
        u4v p0, p1; \
        _Pragma("unroll") for (int i = 0; i < 4; ++i) { p0[i] = pk2(e[2 * i], e[2 * i + 1]); p1[i] = pk2(e[8 + 2 * i], e[8 + 2 * i + 1]); } \
        oacc[t] = mfma32(RC[2], __builtin_bit_cast(bf8, p0), oacc[t]); \
        oacc[t] = mfma32(RC[3], __builtin_bit_cast(bf8, p1), oacc[t]); \
        SGB(0x8, 1); SGB(0x400, 5); SGB(0x8, 1); SGB(0x400, 3); SGB(0x2, 4); SGB(0x400, 3); SGB(0x8, 1); \
        SGB(0x400, 5); SGB(0x2, 4); SGB(0x8, 1); \
        sc = sn; } } while (0)
    LDKV(kA, 0); LDKV(kB, 1);
    f16v sc = QK(kA, 0);
    for (int it = 0; it < 15; it += 3) {
      LDKV(kC, it + 2); __builtin_amdgcn_sched_barrier(0); STEP(kA, kB);
      LDKV(kA, it + 3); __builtin_amdgcn_sched_barrier(0); STEP(kB, kC);
      LDKV(kB, it + 4); __builtin_amdgcn_sched_barrier(0); STEP(kC, kA);
    }
    STEP(kA, kA);
#undef QK
#undef LDKV
#undef STEP
    STAMP(ws, 1, 2);
    if (attempt) __syncthreads();
#pragma unroll
    for (int t = 0; t < QT; ++t) {
#pragma unroll
      for (int rq = 0; rq < 4; ++rq) {
        f4v v = {oacc[t][4 * rq], oacc[t][4 * rq + 1], oacc[t][4 * rq + 2], oacc[t][4 * rq + 3]};
        *(f4v*)(ol + ((((split * 4 + qg * QT + t) * 4 + rq) * 64) + lane) * 4) = v;
      }
    }
    if (tid == 0) flag_l[0] = 0;
    __syncthreads();
    constexpr int NU = 16 / (NT / 64);
    int bad = 0;
#pragma unroll
    for (int uu = 0; uu < NU; ++uu) {
      const int unit = wave * NU + uu, qt = unit >> 2, rq = unit & 3;
      f4v a = {0.f, 0.f, 0.f, 0.f};
      float l = 0.f;
#pragma unroll
      for (int sp = 0; sp < 4; ++sp) {
        a += *(const f4v*)(ol + ((((sp * 4 + qt) * 4 + rq) * 64) + lane) * 4);
        l += ol[((((sp * 4 + qt) * 4 + 3) * 64) + (l31 + 32)) * 4];
      }
      if (!(l >= L_THRESH && l < INFINITY)) bad = 1;
      const float inv = 1.0f / l;
      const int d0 = 8 * rq + 4 * hh;
      if (d0 < DH_) {
        const int k0 = head * DH_ + d0;
        *(u2v*)(ws + OFF_O + ((size_t)(((qblk * 4 + qt) * 28 + (k0 >> 4)) * 64 + ((k0 >> 3) & 1) * 32 + l31) * 16 + (k0 & 7) * 2)) =
            (u2v){pk2(a[0] * inv, a[1] * inv), pk2(a[2] * inv, a[3] * inv)};
      }
    }
    STAMP(ws, 1, 3);
    if (attempt == 1) break;
    if (__any(bad) && lane == 0) atomicOr(flag_l, 1);
    __syncthreads();
    if (flag_l[0] == 0) break;
#pragma unroll
    for (int t = 0; t < QT; ++t) {
      u4v c = __builtin_bit_cast(u4v, qf[t][1]);
      if (hh == 1) c[2] = 0u;
      const bf8 q1 = __builtin_bit_cast(bf8, c);
      float mx = -INFINITY;
      const char* ka = ws + OFF_K + ((size_t)(head * 64) * 2) * 1024 + lane * 16;
      for (int kt = 0; kt < 64; ++kt) {
        const bf8 k0 = *(const bf8*)(ka + kt * 2048), k1 = *(const bf8*)(ka + kt * 2048 + 1024);
        f16v sc = mfma32(k0, qf[t][0], (f16v){});
        sc = mfma32(k1, q1, sc);
#pragma unroll
        for (int r = 0; r < 16; ++r) mx = fmaxf(mx, sc[r]);
      }
      mq[t] = fmaxf(mx, swap32(mx));
    }
  }
}

constexpr int K3_LDS = 2 * 448 * 64 + 16;
__global__ __launch_bounds__(256, 1) void k3_out(const u16* __restrict__ x, const u16* __restrict__ wo,
                                                 const char* __restrict__ ws, u16* __restrict__ out) {
  extern __shared__ __attribute__((aligned(16))) char smem[];
  const int tid = threadIdx.x, lane = tid & 63, wave = tid >> 6;
  const int l31 = lane & 31, hh = lane >> 5;
  const int b = blockIdx.x, xcd = b & 7, j = b >> 3;
  const int mblk = xcd * 4 + (j & 3), nblk = j >> 2;
  const int tw = wave & 1, nw = wave >> 1;
  const int tok = mblk * 64 + tw * 32 + l31;
  STAMP((char*)ws, 2, 0);
  bf8 of[28];
  {
    const char* ob = ws + OFF_O + (size_t)((mblk * 2 + tw) * 28) * 1024 + lane * 16;
#pragma unroll
    for (int s = 0; s < 28; ++s) of[s] = *(const bf8*)(ob + s * 1024);
  }
  {
    u4v tmp[14];
#pragma unroll
    for (int i = 0; i < 14; ++i) {
      const int idx = tid + 256 * i, k = idx >> 3, c = idx & 7;
      tmp[i] = *(const u4v*)(wo + (size_t)k * D_ + nblk * 64 + c * 8);
    }
#pragma unroll
    for (int i = 0; i < 14; ++i) {
      const int idx = tid + 256 * i, k = idx >> 3, c = idx & 7;
      *(u4v*)(smem + (c >> 2) * (448 * 64) + k * 64 + (c & 3) * 16) = tmp[i];
    }
  }
  const int n0 = nblk * 64 + nw * 32;
  u2v xv[4];
#pragma unroll
  for (int g = 0; g < 4; ++g) xv[g] = *(const u2v*)(x + (size_t)tok * D_ + n0 + 8 * g + 4 * hh);
  const f2v st = *(const f2v*)(ws + OFF_STATS + (size_t)tok * 8);
  const int sel = ((const int*)(ws + OFF_CTRL))[1];
  STAMP((char*)ws, 2, 1);
  __syncthreads();
  STAMP((char*)ws, 2, 2);
  const char* trb = smem + nw * (448 * 64) + (8 * hh + ((lane & 15) >> 2)) * 64 + (16 * ((lane >> 4) & 1) + 4 * (lane & 3)) * 2;
  f16v acc = {};
#pragma unroll
  for (int s = 0; s < 28; ++s) {
    s4v a = __builtin_amdgcn_ds_read_tr16_b64_v4i16((lds_s4v*)(trb + s * 16 * 64));
    s4v c = __builtin_amdgcn_ds_read_tr16_b64_v4i16((lds_s4v*)(trb + s * 16 * 64 + 4 * 64));
    typedef __attribute__((ext_vector_type(8))) short s8v;
    const s8v w = __builtin_shufflevector(a, c, 0, 1, 2, 3, 4, 5, 6, 7);
    acc = mfma32(__builtin_bit_cast(bf8, w), of[s], acc);
  }
  STAMP((char*)ws, 2, 3);
  const bool issel = (tok == sel);
#pragma unroll
  for (int g = 0; g < 4; ++g) {
    float r[4];
#pragma unroll
    for (int i = 0; i < 4; ++i) {
      const unsigned xw = xv[g][i >> 1];
      const float xe = (i & 1) ? bfhi(xw) : bflo(xw);
      float xn = bf16r(bf16r(xe - st[0]) * st[1]);
      if (issel) xn = bf16r(xn + ((const float*)(ws + OFF_Y))[n0 + 8 * g + 4 * hh + i]);
      const float gt = bf16r(xn / (1.0f + __expf(-xn)));
      const float sv = bf16r(acc[4 * g + i]);
      r[i] = xe + bf16r(sv * gt);
    }
    *(u2v*)(out + (size_t)tok * D_ + n0 + 8 * g + 4 * hh) = (u2v){pk2(r[0], r[1]), pk2(r[2], r[3])};
  }
  STAMP((char*)ws, 2, 4);
}

#define K2_QT 4

extern "C" void kernel_launch(void* const* d_in, const int* in_sizes, int n_in, void* d_out, int out_size,
                              void* d_ws, size_t ws_size, hipStream_t stream) {
  const u16* x = (const u16*)d_in[0];
  const u16* wqkv = (const u16*)d_in[1];
  const u16* wo = (const u16*)d_in[2];
  const u16* wr = (const u16*)d_in[4];
  const u16* wg = (const u16*)d_in[5];
  const u16* wu = (const u16*)d_in[6];
  const u16* wd = (const u16*)d_in[7];
  char* ws = (char*)d_ws;
  hipFuncSetAttribute((const void*)k1_lnqkv, hipFuncAttributeMaxDynamicSharedMemorySize, K1_LDS);
  hipFuncSetAttribute((const void*)k2_attn<K2_QT>, hipFuncAttributeMaxDynamicSharedMemorySize, 65536 + 64);
  hipLaunchKernelGGL(k1_lnqkv, dim3(256), dim3(512), K1_LDS, stream, x, wqkv, wr, ws);
  constexpr int NT2 = (4 / K2_QT) * 4 * 64;
  hipLaunchKernelGGL(k2_attn<K2_QT>, dim3(256 + NFFN), dim3(NT2), 65536 + 64, stream, x, wg, wu, wd, ws);
  hipLaunchKernelGGL(k3_out, dim3(224), dim3(256), K3_LDS, stream, x, wo, ws, (u16*)d_out);
}
```
